```python
import torch

def _softmax(x, axis=-1):
    return torch.softmax(x, dim=axis)
def _gelu(x):
    return torch.nn.functional.gelu(x, approximate="tanh")
def _silu(x):
    return torch.nn.functional.silu(x)
def _sigmoid(x):
    return torch.sigmoid(x)
def _layernorm(x, axis=-1, eps=1e-5):
    m = x.mean(dim=axis, keepdim=True)
    v = x.var(dim=axis, keepdim=True, unbiased=False)
    return (x - m) / torch.sqrt(v + eps)
def _scatter_add(tgt, idx, src, axis=0):
    return tgt.clone().index_add_(axis, idx, src)
def _segment_sum(src, seg, n):
    out = torch.zeros((n,) + src.shape[1:], dtype=src.dtype)
    return out.index_add_(0, seg, src)
def _pad(x, pw):
    flat = tuple(v for pair in reversed(pw) for v in pair)
    return torch.nn.functional.pad(x, flat)

kernel_name = 'standalone__fused_io_aware_attn__prompt'

def setup_inputs():

    _w = (lambda a: (lambda t: t.to(torch.bfloat16) if t.is_floating_point() else t)(torch.as_tensor(a)))
    B, H, S = 2, 32, 2048
    Dh = max(1, 512 // 32)
    return {
        "q": _w((lambda *s: torch.randn(*s, dtype=torch.bfloat16) * 0.8)(B, H, S, Dh)),
        "k": _w((lambda *s: torch.randn(*s, dtype=torch.bfloat16) * 0.8)(B, H, S, Dh)),
        "v": _w((lambda *s: torch.randn(*s, dtype=torch.bfloat16) * 0.05)(B, H, S, Dh)),
    }

def reference(q, k, v):
    scale = q.shape[-1] ** -0.5
    s = torch.einsum('bhqd,bhkd->bhqk', q, k) * scale
    p = _softmax(s, axis=-1)
    return torch.einsum('bhqk,bhkd->bhqd', p, v)
```

```cpp
#include <hip/hip_runtime.h>
#include <hip/hip_bf16.h>

typedef __bf16   bf16x8  __attribute__((ext_vector_type(8)));
typedef __bf16   bf16x16 __attribute__((ext_vector_type(16)));
typedef __bf16   bf16x2  __attribute__((ext_vector_type(2)));
typedef short    s16x4   __attribute__((ext_vector_type(4)));
typedef short    s16x8   __attribute__((ext_vector_type(8)));
typedef float    f32x2   __attribute__((ext_vector_type(2)));
typedef float    f32x8   __attribute__((ext_vector_type(8)));
typedef float    f32x16  __attribute__((ext_vector_type(16)));
typedef unsigned u32x2   __attribute__((ext_vector_type(2)));
typedef unsigned u32x4   __attribute__((ext_vector_type(4)));
#define LDS_AS __attribute__((address_space(3)))

#define DI __device__ __forceinline__
#define SBAR() __builtin_amdgcn_sched_barrier(0)
#define STAMP(i)
#define RSTAMP(i)
#define DIAGARG
#define DIAGPASS
#define DIAGOUT()

constexpr int SEQ = 2048, DH = 16, NHEADS = 64;
constexpr int NT = 9 * 64;
constexpr int UPI = 8;
#define WG_AHEAD 3
constexpr int LDS_K = 0, LDS_V = 65536, LDS_C = 131072, CSPAN = (UPI + 2) * 1024, LDS_F = LDS_C + CSPAN;
constexpr int LDS_BYTES = LDS_F + 16;
constexpr float QSCALE = 0.25f * 1.4426950408889634f;

DI unsigned cvtpk(float lo, float hi) {
  f32x2 v = {lo, hi};
  return __builtin_bit_cast(unsigned, __builtin_convertvector(v, bf16x2));
}
typedef LDS_AS char* lptr;
DI bf16x8 kread(lptr a) { return *(const LDS_AS bf16x8*)a; }
DI bf16x8 vread(lptr a) {
  s16x4 x = __builtin_amdgcn_ds_read_tr16_b64_v4i16((LDS_AS s16x4*)a);
  s16x4 y = __builtin_amdgcn_ds_read_tr16_b64_v4i16((LDS_AS s16x4*)(a + 128));
  s16x8 r = __builtin_shufflevector(x, y, 0, 1, 2, 3, 4, 5, 6, 7);
  return __builtin_bit_cast(bf16x8, r);
}
DI f32x16 qk(bf16x8 k, bf16x8 q, f32x16 c) { return __builtin_amdgcn_mfma_f32_32x32x16_bf16(k, q, c, 0, 0, 0); }
DI f32x16 mm8(u32x2 a, u32x2 b) {
  return __builtin_amdgcn_mfma_f32_32x32x8bf16_1k(__builtin_bit_cast(s16x4, a), __builtin_bit_cast(s16x4, b), f32x16{}, 0, 0, 0);
}
DI f32x16 pv_lo(bf16x8 v, f32x16 p, f32x16 o, int idx) {
  f32x8 h = __builtin_shufflevector(p, p, 0, 1, 2, 3, 4, 5, 6, 7);
  return __builtin_amdgcn_smfmac_f32_32x32x32_bf16(v, __builtin_bit_cast(bf16x16, h), o, idx, 0, 0);
}
DI f32x16 pv_hi(bf16x8 v, f32x16 p, f32x16 o, int idx) {
  f32x8 h = __builtin_shufflevector(p, p, 8, 9, 10, 11, 12, 13, 14, 15);
  return __builtin_amdgcn_smfmac_f32_32x32x32_bf16(v, __builtin_bit_cast(bf16x16, h), o, idx, 0, 0);
}
DI float max3(float a, float b, float c) { return fmaxf(fmaxf(a, b), c); }
DI float max16(const f32x16& s) {
  float a = max3(s[0], s[1], s[2]), b = max3(s[3], s[4], s[5]);
  a = max3(a, s[6], s[7]);   b = max3(b, s[8], s[9]);
  a = max3(a, s[10], s[11]); b = max3(b, s[12], s[13]);
  return max3(max3(a, s[14], s[15]), b, b);
}
DI float rowmax(const f32x16& s) {
  const float m = max16(s);
  auto rr = __builtin_amdgcn_permlane32_swap(__float_as_uint(m), __float_as_uint(m), false, false);
  return fmaxf(__uint_as_float(rr[0]), __uint_as_float(rr[1]));
}
DI float nanmax3(float a, float b, float c) { return __builtin_elementwise_maximum(__builtin_elementwise_maximum(a, b), c); }
DI float nanmax16(const f32x16& s) {
  float a = nanmax3(s[0], s[1], s[2]), b = nanmax3(s[3], s[4], s[5]);
  a = nanmax3(a, s[6], s[7]);   b = nanmax3(b, s[8], s[9]);
  a = nanmax3(a, s[10], s[11]); b = nanmax3(b, s[12], s[13]);
  return __builtin_elementwise_maximum(nanmax3(a, s[14], s[15]), b);
}
template <int A, int B> DI void exps(f32x16& s) {
#pragma unroll
  for (int i = A; i < B; ++i) s[i] = __builtin_amdgcn_exp2f(s[i]);
  SBAR();
}
DI bf16x8 scale_q(u32x4 raw) {
  u32x4 w;
#pragma unroll
  for (int i = 0; i < 4; ++i)
    w[i] = cvtpk(__uint_as_float(raw[i] << 16) * QSCALE, __uint_as_float(raw[i] & 0xffff0000u) * QSCALE);
  return __builtin_bit_cast(bf16x8, w);
}
DI void dma_group(const char* kb, const char* vb, unsigned koff, unsigned voff, unsigned ldsk, unsigned ldsv) {
  unsigned sv;
  asm volatile("s_mov_b32 %0, m0\n\ts_mov_b32 m0, %6\n\ts_nop 0\n\t"
               "global_load_lds_dwordx4 %4, %2\n\t"
               "s_mov_b32 m0, %5\n\ts_nop 0\n\t"
               "global_load_lds_dwordx4 %3, %1 offset:1024\n\t"
               "global_load_lds_dwordx4 %3, %1 offset:2048\n\t"
               "global_load_lds_dwordx4 %3, %1 offset:3072\n\t"
               "global_load_lds_dwordx4 %3, %1\n\t"
               "s_mov_b32 m0, %6\n\ts_nop 0\n\t"
               "global_load_lds_dwordx4 %4, %2 offset:1024\n\t"
               "global_load_lds_dwordx4 %4, %2 offset:2048\n\t"
               "global_load_lds_dwordx4 %4, %2 offset:3072\n\t"
               "s_mov_b32 m0, %0"
               : "=&s"(sv) : "s"(kb), "s"(vb), "v"(koff), "v"(voff), "s"(ldsk), "s"(ldsv) : "memory");
}
DI void await_groups(unsigned& known, unsigned need, unsigned faddr, unsigned scr) {
  asm volatile("s_cmp_ge_u32 %0, %2\n\ts_cbranch_scc1 2f\n"
               "1:\n\tds_read_b32 %1, %3\n\ts_waitcnt lgkmcnt(0)\n\tv_readfirstlane_b32 %0, %1\n\ts_nop 0\n\t"
               "s_cmp_ge_u32 %0, %2\n\ts_cbranch_scc1 2f\n\ts_sleep 1\n\ts_branch 1b\n2:"
               : "+s"(known) : "v"(scr), "s"(need), "v"(faddr) : "scc", "memory");
}

__global__ __launch_bounds__(NT) void attn_fwd(const char* __restrict__ Q, const char* __restrict__ K,
                                               const char* __restrict__ V, char* __restrict__ O DIAGARG) {
  RSTAMP(0); STAMP(2);
  asm volatile("" :: "s"(Q), "s"(K), "s"(V), "s"(O));
  extern __shared__ __attribute__((aligned(16))) char lds[];
  const int tid = threadIdx.x, lane = tid & 63, r32 = lane & 31, hi = lane >> 5;
  const int wid = __builtin_amdgcn_readfirstlane(tid >> 6);
  const int bid = blockIdx.x, jj = bid >> 3;
  const int head = (bid & 7) + 8 * (jj >> 2), part = jj & 3;
  const size_t hb = (size_t)head * SEQ * DH * 2;
  const unsigned lds0 = (unsigned)(uintptr_t)lds;
  const unsigned rowoff = r32 * 32 + hi * 16;
  const char* Kh = K + hb;
  __builtin_amdgcn_s_setprio(3);

  if (__builtin_expect(wid == 0, 1)) {
    const char* Vh = V + hb;
    dma_group(Kh, Vh, rowoff, lane * 16, lds0 + LDS_K, lds0 + LDS_V);
    const unsigned one = (lane & 1) ? 0u : 0x3F80u;
    const u32x4 c = {one, 0u, one, 0u};
    const unsigned ca = lds0 + LDS_C + lane * 16, faddr = lds0 + LDS_F;
#pragma unroll
    for (int i = 0; i < CSPAN / 1024; ++i) *(LDS_AS u32x4*)(ca + i * 1024) = c;
    *(LDS_AS unsigned*)faddr = 0u;
    STAMP(3);
    asm volatile("s_waitcnt lgkmcnt(0)\n\ts_barrier" ::: "memory");
    STAMP(4);
#pragma unroll 1
    for (int n = 1; n < 16; ++n) {
      dma_group(Kh + n * 4096, Vh + n * 4096, rowoff, lane * 16, lds0 + LDS_K + n * 4096, lds0 + LDS_V + n * 4096);
      if (n >= WG_AHEAD)
        asm volatile("s_waitcnt vmcnt(%2)\n\tds_write_b32 %0, %1" :: "v"(faddr), "v"(n - WG_AHEAD + 1), "i"(8 * WG_AHEAD) : "memory");
    }
#pragma unroll
    for (int g = 16 - WG_AHEAD; g < 16; ++g)
      asm volatile("s_waitcnt vmcnt(%2)\n\tds_write_b32 %0, %1" :: "v"(faddr), "v"(g == 15 ? 1024 : g + 1), "i"(8 * (15 - g)) : "memory");
    STAMP(5); STAMP(6); STAMP(7); STAMP(8);
    DIAGOUT();
    return;
  }

  const size_t qb = hb + (size_t)(part * 512 + (wid - 1) * 64) * 32;
  u32x4 qraw0, qraw1, k0raw;
  asm volatile("global_load_dwordx4 %0, %1, %2" : "=v"(qraw0) : "v"(rowoff), "s"(Q + qb) : "memory");
  asm volatile("global_load_dwordx4 %0, %1, %2" : "=v"(k0raw) : "v"(rowoff), "s"(Kh) : "memory");
  asm volatile("global_load_dwordx4 %0, %1, %2 offset:1024" : "=v"(qraw1) : "v"(rowoff), "s"(Q + qb) : "memory");
  STAMP(3);
  u32x2 zz = {0u, 0u}, half = {0xBF000000u, 0u};
  asm volatile("" : "+v"(zz), "+v"(half));
  f32x16 o0, o1, sb, sa;
  asm volatile("s_nop 1\n\tv_mfma_f32_32x32x8_bf16 %0, %3, %3, 0\n\tv_mfma_f32_32x32x8_bf16 %1, %3, %3, 0\n\tv_mfma_f32_32x32x8_bf16 %2, %3, %3, 0\n\ts_nop 11"
               : "=&v"(o0), "=&v"(o1), "=&v"(sb) : "v"(zz));
  const bool cl = (lane & 16) != 0;
  lptr kp0 = (lptr)lds + LDS_K + lane * 16;
  lptr vp0 = (lptr)lds + (cl ? LDS_C + 128 : LDS_V + hi * 256) + (lane & 15) * 8;
  unsigned vstep = cl ? 0 : UPI * 1024;
  int idx = 0xDDDD;
  unsigned faddr = lds0 + LDS_F, known = 0, scr;
  asm volatile("" : "+v"(idx), "+v"(faddr), "+v"(vstep), "+v"(kp0), "+v"(vp0), "=v"(scr));
  asm volatile("s_barrier" ::: "memory");
  STAMP(4);
  asm volatile("s_waitcnt vmcnt(1)" : "+v"(qraw0), "+v"(k0raw) :: "memory");
  STAMP(5);
  const bf16x8 k0 = __builtin_bit_cast(bf16x8, k0raw);
  const bf16x8 q0 = scale_q(qraw0);
  const f32x16 t0 = qk(k0, q0, f32x16{});
  SBAR();
  asm volatile("s_waitcnt vmcnt(0)" : "+v"(qraw1) :: "memory");
  const bf16x8 q1 = scale_q(qraw1);
  const f32x16 t1 = qk(k0, q1, f32x16{});
  SBAR();
  const f32x16 nm0 = mm8(half, u32x2{__float_as_uint(nanmax16(t0)) & 0xffff0000u, 0u});
  SBAR();
  const f32x16 nm1 = mm8(half, u32x2{__float_as_uint(nanmax16(t1)) & 0xffff0000u, 0u});
  sa = qk(k0, q0, nm0);
  SBAR(); STAMP(6);
  await_groups(known, 1, faddr, scr);
  bf16x8 kA = k0, v0A = vread(vp0), v1A = vread(vp0 + 512), kB, v0B, v1B;
  SBAR(); STAMP(7);
  lptr kp = kp0, vp = vp0;
#define UNIT(u, KC, V0C, V1C, KN, V0N, V1N) do { \
    exps<0, 2>(sa); \
    o1 = pv_hi(V1C, sb, o1, idx); SBAR(); \
    KN = kread(kp + ((u) + 1) * 1024); V0N = vread(vp + ((u) + 1) * 1024); SBAR(); \
    V1N = vread(vp + (u) * 1024 + 512); SBAR();    \
    exps<2, 5>(sa); \
    sb = qk(KC, q1, nm1); SBAR(); \
    exps<5, 11>(sa); \
    o0 = pv_lo(V0C, sa, o0, idx); SBAR(); \
    exps<11, 16>(sa); \
    exps<0, 2>(sb); \
    o0 = pv_hi(V1N, sa, o0, idx); SBAR(); \
    exps<2, 5>(sb); \
    sa = qk(KN, q0, nm0); SBAR(); \
    exps<5, 11>(sb); \
    o1 = pv_lo(V0C, sb, o1, idx); SBAR(); \
    exps<11, 16>(sb); \
  } while (0)
#define UNIT_A(u) UNIT(u, kA, v0A, v1A, kB, v0B, v1B)
#define UNIT_B(u) UNIT(u, kB, v0B, v1B, kA, v0A, v1A)
#define PRIO_AT(I, P) asm volatile("s_cmp_lg_u32 %0, %1\n\ts_cbranch_scc1 1f\n\ts_setprio " #P "\n1:" :: "s"(g0), "i"(I) : "scc")
#pragma unroll 1
  for (unsigned g0 = 0; g0 < 16; g0 += 2) {
    PRIO_AT(12, 2); SBAR();
    UNIT_A(0); UNIT_B(1); UNIT_A(2);
    await_groups(known, g0 + 2, faddr, scr); SBAR();
    UNIT_B(3);
    PRIO_AT(14, 1); SBAR();
    UNIT_A(4); UNIT_B(5); UNIT_A(6);
    PRIO_AT(14, 0); SBAR();
    await_groups(known, g0 + 3, faddr, scr); SBAR();
    UNIT_B(7);
    kp += UPI * 1024; vp += vstep;
    asm volatile("" : "+v"(kp), "+v"(vp));
  }
  o1 = pv_hi(v1A, sb, o1, idx);
  STAMP(8);

  {
    float a = nanmax3(fabsf(o0[0]), fabsf(o0[1]), fabsf(o0[2])), b = nanmax3(fabsf(o1[0]), fabsf(o1[1]), fabsf(o1[2]));
    a = nanmax3(a, fabsf(o0[3]), fabsf(o0[4])); b = nanmax3(b, fabsf(o1[3]), fabsf(o1[4]));
    a = nanmax3(a, fabsf(o0[5]), fabsf(o0[6])); b = nanmax3(b, fabsf(o1[5]), fabsf(o1[6]));
    a = nanmax3(a, fabsf(o0[7]), o0[8]);        b = nanmax3(b, fabsf(o1[7]), o1[8]);
    if (__builtin_expect(__any(!(nanmax3(a, b, b) < 0x1p100f)), 0)) {
#pragma unroll 1
      for (int t = 0; t < 2; ++t) {
        const bf16x8 qv = t ? q1 : q0;
        float m = -INFINITY;
        f32x16 acc = {};
        lptr vq = vp0;
#pragma unroll 1
        for (int u = 0; u < 64; ++u) {
          const bf16x8 kg = kread(kp0 + u * 1024);
          const bf16x8 w0 = vread(vq), w1 = vread(vq + 512);
          vq += cl ? 0 : 1024;
          f32x16 s = qk(kg, qv, f32x16{});
          const float mn = fmaxf(m, rowmax(s));
          const float alpha = __builtin_amdgcn_exp2f(m - mn);
          m = mn;
#pragma unroll
          for (int i = 0; i < 16; ++i) { s[i] = __builtin_amdgcn_exp2f(s[i] - mn); acc[i] *= alpha; }
          acc = pv_lo(w0, s, acc, idx);
          acc = pv_hi(w1, s, acc, idx);
        }
        if (t) o1 = acc; else o0 = acc;
      }
    }
  }

  auto finish = [&](const f32x16& o) {
    const float rl = __builtin_amdgcn_rcpf(o[8]);
    unsigned a0 = cvtpk(o[0] * rl, o[1] * rl), a1 = cvtpk(o[2] * rl, o[3] * rl);
    unsigned b0 = cvtpk(o[4] * rl, o[5] * rl), b1 = cvtpk(o[6] * rl, o[7] * rl);
    auto r0 = __builtin_amdgcn_permlane32_swap(a0, b0, false, false);
    auto r1 = __builtin_amdgcn_permlane32_swap(a1, b1, false, false);
    return u32x4{r0[0], r1[0], r0[1], r1[1]};
  };
  const u32x4 w0 = finish(o0), w1 = finish(o1);
  asm volatile("global_store_dwordx4 %0, %1, %3 sc0 sc1\n\tglobal_store_dwordx4 %0, %2, %3 offset:1024 sc0 sc1\n\ts_nop 1"
               :: "v"(rowoff), "v"(w0), "v"(w1), "s"(O + qb) : "memory");
  DIAGOUT();
}

extern "C" void kernel_launch(void* const* d_in, const int* in_sizes, int n_in, void* d_out, int out_size,
                              void* d_ws, size_t ws_size, hipStream_t stream) {
  (void)in_sizes; (void)n_in; (void)out_size; (void)d_ws; (void)ws_size;
  (void)hipFuncSetAttribute((const void*)attn_fwd, hipFuncAttributeMaxDynamicSharedMemorySize, LDS_BYTES);
  attn_fwd<<<dim3(NHEADS * 4), dim3(NT), LDS_BYTES, stream>>>((const char*)d_in[0], (const char*)d_in[1],
                                                              (const char*)d_in[2], (char*)d_out DIAGPASS);
}
```

```cpp
#include <hip/hip_runtime.h>
#include <hip/hip_bf16.h>

typedef __bf16   bf16x8  __attribute__((ext_vector_type(8)));
typedef __bf16   bf16x16 __attribute__((ext_vector_type(16)));
typedef __bf16   bf16x2  __attribute__((ext_vector_type(2)));
typedef short    s16x4   __attribute__((ext_vector_type(4)));
typedef short    s16x8   __attribute__((ext_vector_type(8)));
typedef float    f32x2   __attribute__((ext_vector_type(2)));
typedef float    f32x8   __attribute__((ext_vector_type(8)));
typedef float    f32x16  __attribute__((ext_vector_type(16)));
typedef unsigned u32x4   __attribute__((ext_vector_type(4)));
#define LDS_AS __attribute__((address_space(3)))
typedef LDS_AS char* lptr;
#define DI __device__ __forceinline__

constexpr int NT = 9 * 64;
constexpr int LDS_V = 65536, LDS_C = 131072, LDS_BYTES = 140304;
constexpr float QSCALE = 0.25f * 1.4426950408889634f;

DI unsigned cvtpk(float lo, float hi) {
  f32x2 v = {lo, hi};
  return __builtin_bit_cast(unsigned, __builtin_convertvector(v, bf16x2));
}
DI bf16x8 vread(lptr a) {
  s16x4 x = __builtin_amdgcn_ds_read_tr16_b64_v4i16((LDS_AS s16x4*)a);
  s16x4 y = __builtin_amdgcn_ds_read_tr16_b64_v4i16((LDS_AS s16x4*)(a + 128));
  s16x8 r = __builtin_shufflevector(x, y, 0, 1, 2, 3, 4, 5, 6, 7);
  return __builtin_bit_cast(bf16x8, r);
}

DI void tile_running_max(const char* Q, char* O, unsigned off, lptr lds, int lane) {
  const bf16x8 q = *(const bf16x8*)(Q + off);
  const bool cl = (lane & 16) != 0;
  lptr kp = lds + lane * 16;
  lptr vp = lds + (cl ? LDS_C + 128 : LDS_V + (lane >> 5) * 256) + (lane & 15) * 8;
  float m = -3.0e38f;
  f32x16 o = {};
#pragma unroll 1
  for (int u = 0; u < 64; ++u) {
    const bf16x8 k = *(const LDS_AS bf16x8*)kp, v0 = vread(vp), v1 = vread(vp + 512);
    kp += 1024; vp += cl ? 0 : 1024;
    f32x16 s = __builtin_amdgcn_mfma_f32_32x32x16_bf16(k, q, f32x16{}, 0, 0, 0);
    float t = s[0];
#pragma unroll
    for (int i = 1; i < 16; ++i) t = fmaxf(t, s[i]);
    auto rr = __builtin_amdgcn_permlane32_swap(__float_as_uint(t), __float_as_uint(t), false, false);
    const float mn = fmaxf(m, fmaxf(__uint_as_float(rr[0]), __uint_as_float(rr[1])));
    const float alpha = __builtin_amdgcn_exp2f((m - mn) * QSCALE);
    m = mn;
#pragma unroll
    for (int i = 0; i < 16; ++i) { s[i] = __builtin_amdgcn_exp2f((s[i] - mn) * QSCALE); o[i] *= alpha; }
    const f32x8 lo = __builtin_shufflevector(s, s, 0, 1, 2, 3, 4, 5, 6, 7), hi = __builtin_shufflevector(s, s, 8, 9, 10, 11, 12, 13, 14, 15);
    o = __builtin_amdgcn_smfmac_f32_32x32x32_bf16(v0, __builtin_bit_cast(bf16x16, lo), o, 0xDDDD, 0, 0);
    o = __builtin_amdgcn_smfmac_f32_32x32x32_bf16(v1, __builtin_bit_cast(bf16x16, hi), o, 0xDDDD, 0, 0);
  }
  const float rl = __builtin_amdgcn_rcpf(o[8]);
  const unsigned a0 = cvtpk(o[0] * rl, o[1] * rl), a1 = cvtpk(o[2] * rl, o[3] * rl);
  const unsigned b0 = cvtpk(o[4] * rl, o[5] * rl), b1 = cvtpk(o[6] * rl, o[7] * rl);
  auto r0 = __builtin_amdgcn_permlane32_swap(a0, b0, false, false);
  auto r1 = __builtin_amdgcn_permlane32_swap(a1, b1, false, false);
  *(u32x4*)(O + off) = u32x4{r0[0], r1[0], r0[1], r1[1]};
}

__global__ __launch_bounds__(NT) void attn_fwd(const char* Q, const char* K, const char* V, char* O) {
  extern __shared__ __attribute__((aligned(16))) char lds[];
  unsigned long long bad;
  asm volatile(
      "s_load_dwordx8 s[40:47], %[ka], 0x0\n"
      "v_readfirstlane_b32 s48, v0\n"
      "s_and_b32 s49, %[bid], 7\n"
      "s_lshl_b32 s49, s49, 19\n"
      "s_lshr_b32 s53, %[bid], 3\n"
      "s_lshl_b32 s53, s53, 14\n"
      "s_or_b32 s53, s53, s49\n"
      "v_lshlrev_b32 v1, 5, v0\n"
      "v_bfe_u32 v9, v0, 5, 1\n"
      "s_and_b32 s49, s53, 0xffff0000\n"
      "v_and_b32 v1, 0x3e0, v1\n"
      "s_lshl_b32 s52, s48, 5\n"
      "v_lshl_or_b32 v1, v9, 4, v1\n"
      "s_add_u32 s53, s53, s52\n"
      "s_cmp_eq_u32 s48, 0\n"
      "v_add_u32 v2, s53, v1\n"
      "v_or_b32 v1, s49, v1\n"
      "s_cbranch_scc1 4f\n"
      "s_cmpk_ge_u32 s48, 320\n"
      "s_cbranch_scc1 11f\n"
      "s_waitcnt lgkmcnt(0)\n"
      "global_load_dwordx4 v[132:135], v2, s[40:41] offset:-2048\n"
      "global_load_dwordx4 v[120:123], v1, s[42:43]\n"
      "global_load_dwordx4 v[128:131], v2, s[40:41] offset:-1024\n"
      "s_branch 10f\n"
      "4:\n"
      "v_lshlrev_b32 v3, 4, v0\n"
      "v_mov_b32 v7, 0x22400\n"
      "v_mov_b32 v8, 0\n"
      "v_or_b32 v2, s49, v3\n"
      "ds_write_b32 v7, v8\n"
      "s_mov_b32 s54, 0\n"
      "s_setprio 3\n"
      "s_waitcnt lgkmcnt(0)\n"
      "1:\n"
      "s_mov_b32 m0, s54\n"
      "s_nop 0\n"
      "global_load_lds_dwordx4 v1, s[42:43] offset:0\n"
      "global_load_lds_dwordx4 v1, s[42:43] offset:1024\n"
      "global_load_lds_dwordx4 v1, s[42:43] offset:2048\n"
      "global_load_lds_dwordx4 v1, s[42:43] offset:3072\n"
      "s_add_u32 m0, s54, 0x10000\n"
      "s_addk_i32 s54, 0x1000\n"
      "global_load_lds_dwordx4 v2, s[44:45] offset:0\n"
      "global_load_lds_dwordx4 v2, s[44:45] offset:1024\n"
      "global_load_lds_dwordx4 v2, s[44:45] offset:2048\n"
      "global_load_lds_dwordx4 v2, s[44:45] offset:3072\n"
      "s_add_u32 s42, s42, 0x1000\n"
      "s_addc_u32 s43, s43, 0\n"
      "s_add_u32 s44, s44, 0x1000\n"
      "s_addc_u32 s45, s45, 0\n"
      "s_cmpk_gt_u32 s54, 0x1000\n"
      "s_cbranch_scc1 2f\n"
      "s_barrier\n"
      "v_and_b32 v16, 1, v0\n"
      "v_add_u32 v3, 0x20000, v3\n"
      "v_add_u32 v16, -1, v16\n"
      "v_mov_b32 v17, 0\n"
      "v_and_b32 v16, 0x3f80, v16\n"
      "v_mov_b32 v19, 0\n"
      "v_mov_b32 v18, v16\n"
      "ds_write_b128 v3, v[16:19] offset:0\n"
      "ds_write_b128 v3, v[16:19] offset:1024\n"
      "ds_write_b128 v3, v[16:19] offset:2048\n"
      "ds_write_b128 v3, v[16:19] offset:3072\n"
      "ds_write_b128 v3, v[16:19] offset:4096\n"
      "ds_write_b128 v3, v[16:19] offset:5120\n"
      "ds_write_b128 v3, v[16:19] offset:6144\n"
      "ds_write_b128 v3, v[16:19] offset:7168\n"
      "ds_write_b128 v3, v[16:19] offset:8192\n"
      "s_branch 1b\n"
      "2:\n"
      "v_add_u32 v8, 1, v8\n"
      "s_cmp_lt_u32 s54, 0x10000\n"
      "s_waitcnt vmcnt(8)\n"
      "ds_write_b32 v7, v8\n"
      "s_cbranch_scc1 1b\n"
      "v_mov_b32 v8, 64\n"
      "s_waitcnt vmcnt(0)\n"
      "ds_write_b32 v7, v8\n"
      "s_endpgm\n"
      "s_nop 0\n"
      "s_nop 0\n"
      "s_nop 0\n"
      "s_nop 0\n"
      "s_nop 0\n"
      "s_nop 0\n"
      "s_nop 0\n"
      "s_nop 0\n"
      "11:\n"
      "10:\n"
      "s_setprio 3\n"
      "v_mov_b32 v10, 0\n"
      "v_mov_b32 v11, 0\n"
      "v_mov_b32 v12, -0.5\n"
      "v_mov_b32 v13, -0.5\n"
      "v_mfma_f32_32x32x8_bf16 v[16:31], v[10:11], v[10:11], 0\n"
      "v_and_b32 v3, 63, v0\n"
      "v_and_b32 v8, 16, v0\n"
      "v_lshlrev_b32 v3, 4, v3\n"
      "v_cmp_eq_u32 vcc, 0, v8\n"
      "v_lshlrev_b32 v9, 8, v9\n"
      "v_mov_b32 v4, 0x20080\n"
      "v_or_b32 v9, 0x10000, v9\n"
      "v_mfma_f32_32x32x8_bf16 v[32:47], v[10:11], v[10:11], 0\n"
      "v_cndmask_b32 v4, v4, v9, vcc\n"
      "v_and_b32 v9, 15, v0\n"
      "v_mov_b32 v5, 0x2000\n"
      "v_lshl_add_u32 v4, v9, 3, v4\n"
      "v_cndmask_b32 v5, 0, v5, vcc\n"
      "v_mov_b32 v6, 0xdddd\n"
      "v_mov_b32 v7, 0x22400\n"
      "v_mfma_f32_32x32x8_bf16 v[96:111], v[10:11], v[10:11], 0\n"
      "v_mov_b64 v[140:141], 0\n"
      "v_mov_b64 v[142:143], 0\n"
      "v_mov_b32 v11, 0xc2370000\n"
      "s_mov_b32 s56, 0x3eb8aa3b\n"
      "s_mov_b32 s57, 0xbc000000\n"
      "s_mov_b32 s50, 1\n"
      "s_barrier\n"
      "s_cmpk_lt_u32 s48, 320\n"
      "s_cbranch_scc1 13f\n"
      "s_sleep 16\n"
      "s_waitcnt lgkmcnt(0)\n"
      "global_load_dwordx4 v[132:135], v2, s[40:41] offset:-2048\n"
      "global_load_dwordx4 v[120:123], v1, s[42:43]\n"
      "global_load_dwordx4 v[128:131], v2, s[40:41] offset:-1024\n"
      "13:\n"
      "s_waitcnt vmcnt(2)\n"
      "v_lshlrev_b32 v80, 16, v132\n"
      "v_and_b32 v81, 0xffff0000, v132\n"
      "v_lshlrev_b32 v82, 16, v133\n"
      "v_and_b32 v83, 0xffff0000, v133\n"
      "v_lshlrev_b32 v84, 16, v134\n"
      "v_and_b32 v85, 0xffff0000, v134\n"
      "v_lshlrev_b32 v86, 16, v135\n"
      "v_and_b32 v87, 0xffff0000, v135\n"
      "v_pk_mul_f32 v[80:81], v[80:81], s[56:57] op_sel_hi:[1,0]\n"
      "v_pk_mul_f32 v[82:83], v[82:83], s[56:57] op_sel_hi:[1,0]\n"
      "v_pk_mul_f32 v[84:85], v[84:85], s[56:57] op_sel_hi:[1,0]\n"
      "v_pk_mul_f32 v[86:87], v[86:87], s[56:57] op_sel_hi:[1,0]\n"
      "v_cvt_pk_bf16_f32 v112, v80, v81\n"
      "v_cvt_pk_bf16_f32 v113, v82, v83\n"
      "v_cvt_pk_bf16_f32 v114, v84, v85\n"
      "v_cvt_pk_bf16_f32 v115, v86, v87\n"
      "s_waitcnt vmcnt(0)\n"
      "v_lshlrev_b32 v88, 16, v128\n"
      "v_and_b32 v89, 0xffff0000, v128\n"
      "v_mfma_f32_32x32x16_bf16 v[48:63], v[120:123], v[112:115], 0\n"
      "v_lshlrev_b32 v90, 16, v129\n"
      "v_and_b32 v91, 0xffff0000, v129\n"
      "v_lshlrev_b32 v92, 16, v130\n"
      "v_and_b32 v93, 0xffff0000, v130\n"
      "v_lshlrev_b32 v94, 16, v131\n"
      "v_and_b32 v95, 0xffff0000, v131\n"
      "v_pk_mul_f32 v[88:89], v[88:89], s[56:57] op_sel_hi:[1,0]\n"
      "v_pk_mul_f32 v[90:91], v[90:91], s[56:57] op_sel_hi:[1,0]\n"
      "v_pk_mul_f32 v[92:93], v[92:93], s[56:57] op_sel_hi:[1,0]\n"
      "v_pk_mul_f32 v[94:95], v[94:95], s[56:57] op_sel_hi:[1,0]\n"
      "v_cvt_pk_bf16_f32 v116, v88, v89\n"
      "v_cvt_pk_bf16_f32 v117, v90, v91\n"
      "v_cvt_pk_bf16_f32 v118, v92, v93\n"
      "v_cvt_pk_bf16_f32 v119, v94, v95\n"
      "s_nop 0\n"
      "ds_read_b32 v8, v7\n"
      "v_mfma_f32_32x32x16_bf16 v[64:79], v[120:123], v[116:119], 0\n"
      "v_maximum3_f32 v48, v48, v49, v50\n"
      "v_maximum3_f32 v51, v51, v52, v53\n"
      "v_maximum3_f32 v48, v48, v54, v55\n"
      "v_maximum3_f32 v51, v51, v56, v57\n"
      "v_maximum3_f32 v48, v48, v58, v59\n"
      "v_maximum3_f32 v51, v51, v60, v61\n"
      "v_maximum3_f32 v48, v48, v62, v63\n"
      "v_maximum3_f32 v48, v48, v51, v51\n"
      "v_fma_f32 v48, |v48|, s57, v48\n"
      "v_and_b32 v10, 0xffff0000, v48\n"
      "s_nop 1\n"
      "v_mfma_f32_32x32x8_bf16 v[48:63], v[12:13], v[10:11], 0\n"
      "v_mfma_f32_32x32x16_bf16 v[80:95], v[120:123], v[112:115], v[48:63]\n"
      "v_maximum3_f32 v64, v64, v65, v66\n"
      "v_maximum3_f32 v67, v67, v68, v69\n"
      "v_maximum3_f32 v64, v64, v70, v71\n"
      "v_maximum3_f32 v67, v67, v72, v73\n"
      "v_maximum3_f32 v64, v64, v74, v75\n"
      "v_maximum3_f32 v67, v67, v76, v77\n"
      "v_maximum3_f32 v64, v64, v78, v79\n"
      "v_maximum3_f32 v64, v64, v67, v67\n"
      "v_fma_f32 v64, |v64|, s57, v64\n"
      "v_and_b32 v10, 0xffff0000, v64\n"
      "s_waitcnt lgkmcnt(0)\n"
      "v_readfirstlane_b32 s52, v8\n"
      "v_mfma_f32_32x32x8_bf16 v[64:79], v[12:13], v[10:11], 0\n"
      "28:\n"
      "s_cmp_ge_u32 s52, s50\n"
      "s_cbranch_scc1 29f\n"
      "ds_read_b32 v8, v7\n"
      "s_waitcnt lgkmcnt(0)\n"
      "v_readfirstlane_b32 s52, v8\n"
      "s_branch 28b\n"
      "29:\n"
      "s_min_u32 s51, s52, 12\n"
      "ds_read_b64_tr_b16 v[128:129], v4 offset:0\n"
      "ds_read_b64_tr_b16 v[130:131], v4 offset:128\n"
      "ds_read_b64_tr_b16 v[136:137], v4 offset:512\n"
      "ds_read_b64_tr_b16 v[138:139], v4 offset:640\n"
      "ds_read_b128 v[124:127], v3 offset:1024\n"
      "30:\n"
      "v_exp_f32 v80, v80\n"
      "v_exp_f32 v81, v81\n"
      "v_smfmac_f32_32x32x32_bf16 v[32:47], v[140:143], v[104:111], v6\n"
      "ds_read_b64_tr_b16 v[132:133], v4 offset:1024\n"
      "v_exp_f32 v82, v82\n"
      "v_exp_f32 v83, v83\n"
      "v_exp_f32 v84, v84\n"
      "v_mfma_f32_32x32x16_bf16 v[96:111], v[120:123], v[116:119], v[64:79]\n"
      "s_waitcnt lgkmcnt(1)\n"
      "ds_read_b64_tr_b16 v[134:135], v4 offset:1152\n"
      "v_exp_f32 v85, v85\n"
      "v_exp_f32 v86, v86\n"
      "v_exp_f32 v87, v87\n"
      "v_exp_f32 v88, v88\n"
      "v_exp_f32 v89, v89\n"
      "v_exp_f32 v90, v90\n"
      "v_smfmac_f32_32x32x32_bf16 v[16:31], v[128:131], v[80:87], v6\n"
      "ds_read_b64_tr_b16 v[140:141], v4 offset:1536\n"
      "v_exp_f32 v91, v91\n"
      "v_exp_f32 v92, v92\n"
      "v_exp_f32 v93, v93\n"
      "v_exp_f32 v94, v94\n"
      "v_exp_f32 v95, v95\n"
      "v_exp_f32 v96, v96\n"
      "v_exp_f32 v97, v97\n"
      "v_smfmac_f32_32x32x32_bf16 v[16:31], v[136:139], v[88:95], v6\n"
      "ds_read_b64_tr_b16 v[142:143], v4 offset:1664\n"
      "v_exp_f32 v98, v98\n"
      "v_exp_f32 v99, v99\n"
      "v_exp_f32 v100, v100\n"
      "v_mfma_f32_32x32x16_bf16 v[80:95], v[124:127], v[112:115], v[48:63]\n"
      "ds_read_b128 v[120:123], v3 offset:2048\n"
      "v_exp_f32 v101, v101\n"
      "v_exp_f32 v102, v102\n"
      "v_exp_f32 v103, v103\n"
      "v_exp_f32 v104, v104\n"
      "v_exp_f32 v105, v105\n"
      "v_exp_f32 v106, v106\n"
      "v_smfmac_f32_32x32x32_bf16 v[32:47], v[128:131], v[96:103], v6\n"
      "v_exp_f32 v107, v107\n"
      "v_exp_f32 v108, v108\n"
      "v_exp_f32 v109, v109\n"
      "v_exp_f32 v110, v110\n"
      "v_exp_f32 v111, v111\n"
      "v_exp_f32 v80, v80\n"
      "v_exp_f32 v81, v81\n"
      "v_smfmac_f32_32x32x32_bf16 v[32:47], v[136:139], v[104:111], v6\n"
      "ds_read_b64_tr_b16 v[128:129], v4 offset:2048\n"
      "v_exp_f32 v82, v82\n"
      "v_exp_f32 v83, v83\n"
      "v_exp_f32 v84, v84\n"
      "v_mfma_f32_32x32x16_bf16 v[96:111], v[124:127], v[116:119], v[64:79]\n"
      "s_waitcnt lgkmcnt(1)\n"
      "ds_read_b64_tr_b16 v[130:131], v4 offset:2176\n"
      "v_exp_f32 v85, v85\n"
      "v_exp_f32 v86, v86\n"
      "v_exp_f32 v87, v87\n"
      "v_exp_f32 v88, v88\n"
      "v_exp_f32 v89, v89\n"
      "v_exp_f32 v90, v90\n"
      "v_smfmac_f32_32x32x32_bf16 v[16:31], v[132:135], v[80:87], v6\n"
      "ds_read_b64_tr_b16 v[136:137], v4 offset:2560\n"
      "v_exp_f32 v91, v91\n"
      "v_exp_f32 v92, v92\n"
      "v_exp_f32 v93, v93\n"
      "v_exp_f32 v94, v94\n"
      "v_exp_f32 v95, v95\n"
      "v_exp_f32 v96, v96\n"
      "v_exp_f32 v97, v97\n"
      "v_smfmac_f32_32x32x32_bf16 v[16:31], v[140:143], v[88:95], v6\n"
      "ds_read_b64_tr_b16 v[138:139], v4 offset:2688\n"
      "v_exp_f32 v98, v98\n"
      "v_exp_f32 v99, v99\n"
      "v_exp_f32 v100, v100\n"
      "v_mfma_f32_32x32x16_bf16 v[80:95], v[120:123], v[112:115], v[48:63]\n"
      "ds_read_b128 v[124:127], v3 offset:3072\n"
      "v_exp_f32 v101, v101\n"
      "v_exp_f32 v102, v102\n"
      "v_exp_f32 v103, v103\n"
      "v_exp_f32 v104, v104\n"
      "v_exp_f32 v105, v105\n"
      "v_exp_f32 v106, v106\n"
      "v_smfmac_f32_32x32x32_bf16 v[32:47], v[132:135], v[96:103], v6\n"
      "s_add_u32 s50, s50, 1\n"
      "s_cmp_ge_u32 s51, s50\n"
      "s_cbranch_scc0 41f\n"
      "31:\n"
      "v_exp_f32 v107, v107\n"
      "v_exp_f32 v108, v108\n"
      "v_exp_f32 v109, v109\n"
      "v_exp_f32 v110, v110\n"
      "v_exp_f32 v111, v111\n"
      "v_exp_f32 v80, v80\n"
      "v_exp_f32 v81, v81\n"
      "v_smfmac_f32_32x32x32_bf16 v[32:47], v[140:143], v[104:111], v6\n"
      "ds_read_b64_tr_b16 v[132:133], v4 offset:3072\n"
      "v_exp_f32 v82, v82\n"
      "v_exp_f32 v83, v83\n"
      "v_exp_f32 v84, v84\n"
      "v_mfma_f32_32x32x16_bf16 v[96:111], v[120:123], v[116:119], v[64:79]\n"
      "s_waitcnt lgkmcnt(1)\n"
      "ds_read_b64_tr_b16 v[134:135], v4 offset:3200\n"
      "v_exp_f32 v85, v85\n"
      "v_exp_f32 v86, v86\n"
      "v_exp_f32 v87, v87\n"
      "v_exp_f32 v88, v88\n"
      "v_exp_f32 v89, v89\n"
      "v_exp_f32 v90, v90\n"
      "v_smfmac_f32_32x32x32_bf16 v[16:31], v[128:131], v[80:87], v6\n"
      "ds_read_b64_tr_b16 v[140:141], v4 offset:3584\n"
      "v_exp_f32 v91, v91\n"
      "v_exp_f32 v92, v92\n"
      "v_exp_f32 v93, v93\n"
      "v_exp_f32 v94, v94\n"
      "v_exp_f32 v95, v95\n"
      "v_exp_f32 v96, v96\n"
      "v_exp_f32 v97, v97\n"
      "v_smfmac_f32_32x32x32_bf16 v[16:31], v[136:139], v[88:95], v6\n"
      "ds_read_b64_tr_b16 v[142:143], v4 offset:3712\n"
      "v_exp_f32 v98, v98\n"
      "v_exp_f32 v99, v99\n"
      "v_exp_f32 v100, v100\n"
      "v_mfma_f32_32x32x16_bf16 v[80:95], v[124:127], v[112:115], v[48:63]\n"
      "ds_read_b128 v[120:123], v3 offset:4096\n"
      "v_exp_f32 v101, v101\n"
      "v_exp_f32 v102, v102\n"
      "v_exp_f32 v103, v103\n"
      "v_exp_f32 v104, v104\n"
      "v_exp_f32 v105, v105\n"
      "v_exp_f32 v106, v106\n"
      "v_smfmac_f32_32x32x32_bf16 v[32:47], v[128:131], v[96:103], v6\n"
      "v_exp_f32 v107, v107\n"
      "v_exp_f32 v108, v108\n"
      "v_exp_f32 v109, v109\n"
      "v_exp_f32 v110, v110\n"
      "v_exp_f32 v111, v111\n"
      "v_exp_f32 v80, v80\n"
      "v_exp_f32 v81, v81\n"
      "v_smfmac_f32_32x32x32_bf16 v[32:47], v[136:139], v[104:111], v6\n"
      "ds_read_b64_tr_b16 v[128:129], v4 offset:4096\n"
      "v_exp_f32 v82, v82\n"
      "v_exp_f32 v83, v83\n"
      "v_exp_f32 v84, v84\n"
      "v_mfma_f32_32x32x16_bf16 v[96:111], v[124:127], v[116:119], v[64:79]\n"
      "s_waitcnt lgkmcnt(1)\n"
      "ds_read_b64_tr_b16 v[130:131], v4 offset:4224\n"
      "v_exp_f32 v85, v85\n"
      "v_exp_f32 v86, v86\n"
      "v_exp_f32 v87, v87\n"
      "v_exp_f32 v88, v88\n"
      "v_exp_f32 v89, v89\n"
      "v_exp_f32 v90, v90\n"
      "v_smfmac_f32_32x32x32_bf16 v[16:31], v[132:135], v[80:87], v6\n"
      "ds_read_b64_tr_b16 v[136:137], v4 offset:4608\n"
      "v_exp_f32 v91, v91\n"
      "v_exp_f32 v92, v92\n"
      "v_exp_f32 v93, v93\n"
      "v_exp_f32 v94, v94\n"
      "v_exp_f32 v95, v95\n"
      "v_exp_f32 v96, v96\n"
      "v_exp_f32 v97, v97\n"
      "v_smfmac_f32_32x32x32_bf16 v[16:31], v[140:143], v[88:95], v6\n"
      "ds_read_b64_tr_b16 v[138:139], v4 offset:4736\n"
      "v_exp_f32 v98, v98\n"
      "v_exp_f32 v99, v99\n"
      "v_exp_f32 v100, v100\n"
      "v_mfma_f32_32x32x16_bf16 v[80:95], v[120:123], v[112:115], v[48:63]\n"
      "ds_read_b128 v[124:127], v3 offset:5120\n"
      "v_exp_f32 v101, v101\n"
      "v_exp_f32 v102, v102\n"
      "v_exp_f32 v103, v103\n"
      "v_exp_f32 v104, v104\n"
      "v_exp_f32 v105, v105\n"
      "v_exp_f32 v106, v106\n"
      "v_smfmac_f32_32x32x32_bf16 v[32:47], v[132:135], v[96:103], v6\n"
      "v_exp_f32 v107, v107\n"
      "v_exp_f32 v108, v108\n"
      "v_exp_f32 v109, v109\n"
      "v_exp_f32 v110, v110\n"
      "v_exp_f32 v111, v111\n"
      "v_exp_f32 v80, v80\n"
      "v_exp_f32 v81, v81\n"
      "v_smfmac_f32_32x32x32_bf16 v[32:47], v[140:143], v[104:111], v6\n"
      "ds_read_b64_tr_b16 v[132:133], v4 offset:5120\n"
      "v_exp_f32 v82, v82\n"
      "v_exp_f32 v83, v83\n"
      "v_exp_f32 v84, v84\n"
      "v_mfma_f32_32x32x16_bf16 v[96:111], v[120:123], v[116:119], v[64:79]\n"
      "s_waitcnt lgkmcnt(1)\n"
      "ds_read_b64_tr_b16 v[134:135], v4 offset:5248\n"
      "v_exp_f32 v85, v85\n"
      "v_exp_f32 v86, v86\n"
      "v_exp_f32 v87, v87\n"
      "v_exp_f32 v88, v88\n"
      "v_exp_f32 v89, v89\n"
      "v_exp_f32 v90, v90\n"
      "v_smfmac_f32_32x32x32_bf16 v[16:31], v[128:131], v[80:87], v6\n"
      "ds_read_b64_tr_b16 v[140:141], v4 offset:5632\n"
      "v_exp_f32 v91, v91\n"
      "v_exp_f32 v92, v92\n"
      "v_exp_f32 v93, v93\n"
      "v_exp_f32 v94, v94\n"
      "v_exp_f32 v95, v95\n"
      "v_exp_f32 v96, v96\n"
      "v_exp_f32 v97, v97\n"
      "v_smfmac_f32_32x32x32_bf16 v[16:31], v[136:139], v[88:95], v6\n"
      "ds_read_b64_tr_b16 v[142:143], v4 offset:5760\n"
      "v_exp_f32 v98, v98\n"
      "v_exp_f32 v99, v99\n"
      "v_exp_f32 v100, v100\n"
      "v_mfma_f32_32x32x16_bf16 v[80:95], v[124:127], v[112:115], v[48:63]\n"
      "ds_read_b128 v[120:123], v3 offset:6144\n"
      "v_exp_f32 v101, v101\n"
      "v_exp_f32 v102, v102\n"
      "v_exp_f32 v103, v103\n"
      "v_exp_f32 v104, v104\n"
      "v_exp_f32 v105, v105\n"
      "v_exp_f32 v106, v106\n"
      "v_smfmac_f32_32x32x32_bf16 v[32:47], v[128:131], v[96:103], v6\n"
      "v_exp_f32 v107, v107\n"
      "v_exp_f32 v108, v108\n"
      "v_exp_f32 v109, v109\n"
      "v_exp_f32 v110, v110\n"
      "v_exp_f32 v111, v111\n"
      "v_exp_f32 v80, v80\n"
      "v_exp_f32 v81, v81\n"
      "v_smfmac_f32_32x32x32_bf16 v[32:47], v[136:139], v[104:111], v6\n"
      "ds_read_b64_tr_b16 v[128:129], v4 offset:6144\n"
      "v_exp_f32 v82, v82\n"
      "v_exp_f32 v83, v83\n"
      "v_exp_f32 v84, v84\n"
      "v_mfma_f32_32x32x16_bf16 v[96:111], v[124:127], v[116:119], v[64:79]\n"
      "s_waitcnt lgkmcnt(1)\n"
      "ds_read_b64_tr_b16 v[130:131], v4 offset:6272\n"
      "v_exp_f32 v85, v85\n"
      "v_exp_f32 v86, v86\n"
      "v_exp_f32 v87, v87\n"
      "v_exp_f32 v88, v88\n"
      "v_exp_f32 v89, v89\n"
      "v_exp_f32 v90, v90\n"
      "v_smfmac_f32_32x32x32_bf16 v[16:31], v[132:135], v[80:87], v6\n"
      "ds_read_b64_tr_b16 v[136:137], v4 offset:6656\n"
      "v_exp_f32 v91, v91\n"
      "v_exp_f32 v92, v92\n"
      "v_exp_f32 v93, v93\n"
      "v_exp_f32 v94, v94\n"
      "v_exp_f32 v95, v95\n"
      "v_exp_f32 v96, v96\n"
      "v_exp_f32 v97, v97\n"
      "v_smfmac_f32_32x32x32_bf16 v[16:31], v[140:143], v[88:95], v6\n"
      "ds_read_b64_tr_b16 v[138:139], v4 offset:6784\n"
      "v_exp_f32 v98, v98\n"
      "v_exp_f32 v99, v99\n"
      "v_exp_f32 v100, v100\n"
      "v_mfma_f32_32x32x16_bf16 v[80:95], v[120:123], v[112:115], v[48:63]\n"
      "ds_read_b128 v[124:127], v3 offset:7168\n"
      "v_exp_f32 v101, v101\n"
      "v_exp_f32 v102, v102\n"
      "v_exp_f32 v103, v103\n"
      "v_exp_f32 v104, v104\n"
      "v_exp_f32 v105, v105\n"
      "v_exp_f32 v106, v106\n"
      "v_smfmac_f32_32x32x32_bf16 v[32:47], v[132:135], v[96:103], v6\n"
      "s_add_u32 s50, s50, 1\n"
      "s_cmp_ge_u32 s51, s50\n"
      "s_cbranch_scc0 42f\n"
      "32:\n"
      "v_exp_f32 v107, v107\n"
      "v_exp_f32 v108, v108\n"
      "v_exp_f32 v109, v109\n"
      "v_exp_f32 v110, v110\n"
      "v_exp_f32 v111, v111\n"
      "v_exp_f32 v80, v80\n"
      "v_exp_f32 v81, v81\n"
      "v_smfmac_f32_32x32x32_bf16 v[32:47], v[140:143], v[104:111], v6\n"
      "ds_read_b64_tr_b16 v[132:133], v4 offset:7168\n"
      "v_exp_f32 v82, v82\n"
      "v_exp_f32 v83, v83\n"
      "v_exp_f32 v84, v84\n"
      "v_mfma_f32_32x32x16_bf16 v[96:111], v[120:123], v[116:119], v[64:79]\n"
      "s_waitcnt lgkmcnt(1)\n"
      "ds_read_b64_tr_b16 v[134:135], v4 offset:7296\n"
      "v_exp_f32 v85, v85\n"
      "v_exp_f32 v86, v86\n"
      "v_exp_f32 v87, v87\n"
      "v_exp_f32 v88, v88\n"
      "v_exp_f32 v89, v89\n"
      "v_exp_f32 v90, v90\n"
      "v_smfmac_f32_32x32x32_bf16 v[16:31], v[128:131], v[80:87], v6\n"
      "ds_read_b64_tr_b16 v[140:141], v4 offset:7680\n"
      "v_exp_f32 v91, v91\n"
      "v_exp_f32 v92, v92\n"
      "v_exp_f32 v93, v93\n"
      "v_exp_f32 v94, v94\n"
      "v_exp_f32 v95, v95\n"
      "v_exp_f32 v96, v96\n"
      "v_exp_f32 v97, v97\n"
      "v_smfmac_f32_32x32x32_bf16 v[16:31], v[136:139], v[88:95], v6\n"
      "ds_read_b64_tr_b16 v[142:143], v4 offset:7808\n"
      "v_exp_f32 v98, v98\n"
      "v_exp_f32 v99, v99\n"
      "v_exp_f32 v100, v100\n"
      "v_mfma_f32_32x32x16_bf16 v[80:95], v[124:127], v[112:115], v[48:63]\n"
      "ds_read_b128 v[120:123], v3 offset:8192\n"
      "v_exp_f32 v101, v101\n"
      "v_exp_f32 v102, v102\n"
      "v_exp_f32 v103, v103\n"
      "v_exp_f32 v104, v104\n"
      "v_exp_f32 v105, v105\n"
      "v_exp_f32 v106, v106\n"
      "v_smfmac_f32_32x32x32_bf16 v[32:47], v[128:131], v[96:103], v6\n"
      "v_exp_f32 v107, v107\n"
      "v_exp_f32 v108, v108\n"
      "v_exp_f32 v109, v109\n"
      "v_exp_f32 v110, v110\n"
      "v_exp_f32 v111, v111\n"
      "v_exp_f32 v80, v80\n"
      "v_exp_f32 v81, v81\n"
      "v_smfmac_f32_32x32x32_bf16 v[32:47], v[136:139], v[104:111], v6\n"
      "ds_read_b64_tr_b16 v[128:129], v4 offset:8192\n"
      "v_exp_f32 v82, v82\n"
      "v_exp_f32 v83, v83\n"
      "v_exp_f32 v84, v84\n"
      "v_mfma_f32_32x32x16_bf16 v[96:111], v[124:127], v[116:119], v[64:79]\n"
      "s_waitcnt lgkmcnt(1)\n"
      "ds_read_b64_tr_b16 v[130:131], v4 offset:8320\n"
      "v_exp_f32 v85, v85\n"
      "v_exp_f32 v86, v86\n"
      "v_exp_f32 v87, v87\n"
      "v_exp_f32 v88, v88\n"
      "v_exp_f32 v89, v89\n"
      "v_exp_f32 v90, v90\n"
      "v_smfmac_f32_32x32x32_bf16 v[16:31], v[132:135], v[80:87], v6\n"
      "ds_read_b64_tr_b16 v[136:137], v4 offset:8704\n"
      "v_exp_f32 v91, v91\n"
      "v_exp_f32 v92, v92\n"
      "v_exp_f32 v93, v93\n"
      "v_exp_f32 v94, v94\n"
      "v_exp_f32 v95, v95\n"
      "v_exp_f32 v96, v96\n"
      "v_exp_f32 v97, v97\n"
      "v_smfmac_f32_32x32x32_bf16 v[16:31], v[140:143], v[88:95], v6\n"
      "ds_read_b64_tr_b16 v[138:139], v4 offset:8832\n"
      "v_exp_f32 v98, v98\n"
      "v_exp_f32 v99, v99\n"
      "v_exp_f32 v100, v100\n"
      "v_mfma_f32_32x32x16_bf16 v[80:95], v[120:123], v[112:115], v[48:63]\n"
      "ds_read_b128 v[124:127], v3 offset:9216\n"
      "v_exp_f32 v101, v101\n"
      "v_exp_f32 v102, v102\n"
      "v_exp_f32 v103, v103\n"
      "v_exp_f32 v104, v104\n"
      "v_exp_f32 v105, v105\n"
      "v_exp_f32 v106, v106\n"
      "v_smfmac_f32_32x32x32_bf16 v[32:47], v[132:135], v[96:103], v6\n"
      "s_cmp_lt_u32 s50, 17\n"
      "v_add_u32 v3, 0x2000, v3\n"
      "v_add_u32 v4, v4, v5\n"
      "v_exp_f32 v107, v107\n"
      "v_exp_f32 v108, v108\n"
      "v_exp_f32 v109, v109\n"
      "v_exp_f32 v110, v110\n"
      "v_exp_f32 v111, v111\n"
      "s_cbranch_scc1 30b\n"
      "s_nop 0\n"
      "v_smfmac_f32_32x32x32_bf16 v[32:47], v[140:143], v[104:111], v6\n"
      "v_rcp_f32 v56, v24\n"
      "s_nop 0\n"
      "v_pk_mul_f32 v[48:49], v[16:17], v[56:57] op_sel_hi:[1,0]\n"
      "v_pk_mul_f32 v[50:51], v[18:19], v[56:57] op_sel_hi:[1,0]\n"
      "v_pk_mul_f32 v[52:53], v[20:21], v[56:57] op_sel_hi:[1,0]\n"
      "v_pk_mul_f32 v[54:55], v[22:23], v[56:57] op_sel_hi:[1,0]\n"
      "v_cvt_pk_bf16_f32 v58, v48, v49\n"
      "v_cvt_pk_bf16_f32 v59, v50, v51\n"
      "v_cvt_pk_bf16_f32 v60, v52, v53\n"
      "v_cvt_pk_bf16_f32 v61, v54, v55\n"
      "s_nop 1\n"
      "v_permlane32_swap_b32 v58, v60\n"
      "v_permlane32_swap_b32 v59, v61\n"
      "global_store_dwordx4 v2, v[58:61], s[46:47] offset:-2048 sc0 sc1\n"
      "v_rcp_f32 v72, v40\n"
      "s_nop 0\n"
      "v_pk_mul_f32 v[64:65], v[32:33], v[72:73] op_sel_hi:[1,0]\n"
      "v_pk_mul_f32 v[66:67], v[34:35], v[72:73] op_sel_hi:[1,0]\n"
      "v_pk_mul_f32 v[68:69], v[36:37], v[72:73] op_sel_hi:[1,0]\n"
      "v_pk_mul_f32 v[70:71], v[38:39], v[72:73] op_sel_hi:[1,0]\n"
      "v_cvt_pk_bf16_f32 v74, v64, v65\n"
      "v_cvt_pk_bf16_f32 v75, v66, v67\n"
      "v_cvt_pk_bf16_f32 v76, v68, v69\n"
      "v_cvt_pk_bf16_f32 v77, v70, v71\n"
      "s_nop 1\n"
      "v_permlane32_swap_b32 v74, v76\n"
      "v_permlane32_swap_b32 v75, v77\n"
      "global_store_dwordx4 v2, v[74:77], s[46:47] offset:-1024 sc0 sc1\n"
      "v_maximum3_f32 v9, |v16|, |v17|, |v18|\n"
      "v_maximum3_f32 v8, |v32|, |v33|, |v34|\n"
      "v_maximum3_f32 v9, v9, |v19|, |v20|\n"
      "v_maximum3_f32 v8, v8, |v35|, |v36|\n"
      "v_maximum3_f32 v9, v9, |v21|, |v22|\n"
      "v_maximum3_f32 v8, v8, |v37|, |v38|\n"
      "v_maximum3_f32 v9, v9, |v23|, v24\n"
      "v_maximum3_f32 v8, v8, |v39|, v40\n"
      "v_maximum3_f32 v9, v9, v8, v56\n"
      "s_mov_b32 s53, 0x7b800000\n"
      "v_maximum3_f32 v9, v9, v72, v72\n"
      "v_cmp_ngt_f32 %[bad], s53, v9\n"
      "s_nop 0\n"
      "s_cmp_lg_u64 %[bad], 0\n"
      "s_cbranch_scc1 99f\n"
      "s_endpgm\n"
      "41:\n"
      "s_cmp_lg_u32 s50, 16\n"
      "s_cbranch_scc1 1f\n"
      "s_setprio 1\n"
      "1:\n"
      "2:\n"
      "s_cmp_ge_u32 s52, s50\n"
      "s_cbranch_scc1 3f\n"
      "ds_read_b32 v8, v7\n"
      "s_waitcnt lgkmcnt(0)\n"
      "v_readfirstlane_b32 s52, v8\n"
      "s_nop 0\n"
      "s_cmp_ge_u32 s52, s50\n"
      "s_cbranch_scc1 3f\n"
      "s_sleep 1\n"
      "s_branch 2b\n"
      "3:\n"
      "s_cmp_lt_u32 s50, 16\n"
      "s_cselect_b32 s51, 15, 16\n"
      "s_cmp_lt_u32 s50, 13\n"
      "s_cselect_b32 s51, 12, s51\n"
      "s_min_u32 s51, s51, s52\n"
      "s_branch 31b\n"
      "42:\n"
      "s_cmp_lg_u32 s50, 13\n"
      "s_cbranch_scc1 1f\n"
      "s_setprio 2\n"
      "1:\n"
      "s_cmp_lg_u32 s50, 17\n"
      "s_cbranch_scc1 1f\n"
      "s_setprio 0\n"
      "1:\n"
      "2:\n"
      "s_cmp_ge_u32 s52, s50\n"
      "s_cbranch_scc1 3f\n"
      "ds_read_b32 v8, v7\n"
      "s_waitcnt lgkmcnt(0)\n"
      "v_readfirstlane_b32 s52, v8\n"
      "s_nop 0\n"
      "s_cmp_ge_u32 s52, s50\n"
      "s_cbranch_scc1 3f\n"
      "s_sleep 1\n"
      "s_branch 2b\n"
      "3:\n"
      "s_cmp_lt_u32 s50, 16\n"
      "s_cselect_b32 s51, 15, 16\n"
      "s_cmp_lt_u32 s50, 13\n"
      "s_cselect_b32 s51, 12, s51\n"
      "s_min_u32 s51, s51, s52\n"
      "s_branch 32b\n"
      "99:\n"
      "s_waitcnt vmcnt(0) lgkmcnt(0)\n"
      : [bad] "=&s"(bad)
      : [ka] "s"(__builtin_amdgcn_kernarg_segment_ptr()), [bid] "s"(blockIdx.x), "{v0}"(threadIdx.x)
      : "v1", "v2", "v3", "v4", "v5", "v6", "v7", "v8", "v9", "v10", "v11", "v12", "v13", "v14", "v15", "v16", "v17", "v18", "v19", "v20", "v21",
        "v22", "v23", "v24", "v25", "v26", "v27", "v28", "v29", "v30", "v31", "v32", "v33", "v34", "v35", "v36", "v37", "v38", "v39", "v40", "v41",
        "v42", "v43", "v44", "v45", "v46", "v47", "v48", "v49", "v50", "v51", "v52", "v53", "v54", "v55", "v56", "v57", "v58", "v59", "v60", "v61",
        "v62", "v63", "v64", "v65", "v66", "v67", "v68", "v69", "v70", "v71", "v72", "v73", "v74", "v75", "v76", "v77", "v78", "v79", "v80", "v81",
        "v82", "v83", "v84", "v85", "v86", "v87", "v88", "v89", "v90", "v91", "v92", "v93", "v94", "v95", "v96", "v97", "v98", "v99", "v100", "v101",
        "v102", "v103", "v104", "v105", "v106", "v107", "v108", "v109", "v110", "v111", "v112", "v113", "v114", "v115", "v116", "v117", "v118",
        "v119", "v120", "v121", "v122", "v123", "v124", "v125", "v126", "v127", "v128", "v129", "v130", "v131", "v132", "v133", "v134", "v135",
        "v136", "v137", "v138", "v139", "v140", "v141", "v142", "v143", "s40", "s41", "s42", "s43", "s44", "s45", "s46", "s47", "s48", "s49", "s50",
        "s51", "s52", "s53", "s54", "s55", "s56", "s57", "s58", "s59", "vcc", "scc", "memory");
  if (bad != 0) {
    const int lane = threadIdx.x & 63, wid = threadIdx.x >> 6, bid = blockIdx.x;
    const unsigned head = (bid & 7) * 8 + (bid >> 5), part = (bid >> 3) & 3;
    const unsigned off = head * 65536 + (part * 512 + (wid - 1) * 64 + (lane & 31)) * 32 + (lane >> 5) * 16;
#pragma unroll 1
    for (int t = 0; t < 2; ++t) tile_running_max(Q, O, off + t * 1024, (lptr)lds, lane);
  }
}

extern "C" void kernel_launch(void* const* d_in, const int* in_sizes, int n_in, void* d_out, int out_size,
                              void* d_ws, size_t ws_size, hipStream_t stream) {
  (void)in_sizes; (void)n_in; (void)out_size; (void)d_ws; (void)ws_size;
  (void)hipFuncSetAttribute((const void*)attn_fwd, hipFuncAttributeMaxDynamicSharedMemorySize, LDS_BYTES);
  attn_fwd<<<dim3(256), dim3(NT), LDS_BYTES, stream>>>((const char*)d_in[0], (const char*)d_in[1], (const char*)d_in[2],
                                                       (char*)d_out);
}
```
